```python
import torch

def _softmax(x, axis=-1):
    return torch.softmax(x, dim=axis)

def _gelu(x):
    return torch.nn.functional.gelu(x, approximate='tanh')

def _silu(x):
    return torch.nn.functional.silu(x)

def _sigmoid(x):
    return torch.sigmoid(x)

def _layernorm(x, axis=-1, eps=1e-05):
    m = x.mean(dim=axis, keepdim=True)
    v = x.var(dim=axis, keepdim=True, unbiased=False)
    return (x - m) / torch.sqrt(v + eps)

def _scatter_add(tgt, idx, src, axis=0):
    return tgt.clone().index_add_(axis, idx, src)

def _segment_sum(src, seg, n):
    out = torch.zeros((n,) + src.shape[1:], dtype=src.dtype)
    return out.index_add_(0, seg, src)

def _pad(x, pw):
    flat = tuple((v for pair in reversed(pw) for v in pair))
    return torch.nn.functional.pad(x, flat)
kernel_name = 'concat_heads__sliding_block__memory_aug_recurrent_mem_tr__prompt'

def setup_inputs():

    _w = lambda a: (lambda t: t.to(torch.bfloat16) if t.is_floating_point() else t)(torch.as_tensor(a))

    def _A_setup():
        B, S, D = (1, 2048, 1024)
        return {'x': _w((lambda *s: torch.randn(*s, dtype=torch.bfloat16) * 0.2)(B, S, D)), 'wqkv': _w((lambda *s: torch.randn(*s, dtype=torch.bfloat16) * 0.2)(D, 3 * D)), 'wo': _w((lambda *s: torch.randn(*s, dtype=torch.bfloat16) * 0.05)(D, D))}
    _A_in = _A_setup()

    def _B_setup():
        B, S, Dm = (1, 2048, 1024)
        H, Dh = (32, 128)
        M, seg = (8, 32)
        seg = min(seg, S)
        S = S // seg * seg
        return {'x': _w((lambda *s: torch.randn(*s, dtype=torch.bfloat16) * 0.05)(B, S, Dm)), 'mem0': _w((lambda *s: torch.randn(*s, dtype=torch.bfloat16) * 1.6)(B, M, Dm)), 'Wq': _w((lambda *s: torch.randn(*s, dtype=torch.bfloat16) * 0.8)(Dm, H, Dh) * Dm ** (-0.5)), 'Wk': _w((lambda *s: torch.randn(*s, dtype=torch.bfloat16) * 0.8)(Dm, H, Dh) * Dm ** (-0.5)), 'Wv': _w((lambda *s: torch.randn(*s, dtype=torch.bfloat16) * 1.6)(Dm, H, Dh) * Dm ** (-0.5)), 'Wo': _w((lambda *s: torch.randn(*s, dtype=torch.bfloat16) * 1.6)(H, Dh, Dm) * (H * Dh) ** (-0.5)), 'seg_len': seg}
    _B_in = _B_setup()
    return {'A_x': _A_in['x'], 'A_wqkv': _A_in['wqkv'], 'A_wo': _A_in['wo'], 'B_x': _B_in['x'], 'B_mem0': _B_in['mem0'], 'B_Wq': _B_in['Wq'], 'B_Wk': _B_in['Wk'], 'B_Wv': _B_in['Wv'], 'B_Wo': _B_in['Wo'], 'B_seg_len': _B_in['seg_len']}

def reference(A_x, A_wqkv, A_wo, B_x, B_mem0, B_Wq, B_Wk, B_Wv, B_Wo, B_seg_len):

    def _A_ref(x, wqkv, wo):
        B, S, D = x.shape
        H = 32
        Dh = D // H
        W = 256
        qkv = (lambda x, *d: x.permute(*d))((x @ wqkv).reshape(B, S, 3, H, Dh), 2, 0, 3, 1, 4)
        q, k, v = (qkv[0], qkv[1], qkv[2])
        scores = torch.einsum('bhqd,bhkd->bhqk', q, k) * Dh ** (-0.5)
        idx = torch.arange(S)
        mask = (idx[:, None] - idx[None, :] >= 0) & (idx[:, None] - idx[None, :] < W)
        scores = torch.where(mask[None, None], scores, -1000000000.0)
        p = _softmax(scores, axis=-1)
        o = torch.einsum('bhqk,bhkd->bhqd', p, v)
        return (lambda x, *d: x.permute(*d))(o, 0, 2, 1, 3).reshape(B, S, D) @ wo

    def _B_ref(x, mem0, Wq, Wk, Wv, Wo, seg_len):
        B, S, Dm = x.shape
        H, Dh = (Wq.shape[1], Wq.shape[2])
        M = mem0.shape[1]
        scale = Dh ** (-0.5)
        n_seg = S // seg_len
        L = M + seg_len
        idx = torch.arange(L)
        mask = (idx[:, None] < M) | (idx[:, None] >= idx[None, :])
        mem = mem0
        outs = []
        for s in range(n_seg):
            xs = x[:, s * seg_len:(s + 1) * seg_len, :].clone()
            h = torch.concatenate([mem, xs], axis=1)
            q = torch.einsum('btd,dhe->bhte', h, Wq)
            k = torch.einsum('btd,dhe->bhte', h, Wk)
            v = torch.einsum('btd,dhe->bhte', h, Wv)
            scores = torch.einsum('bhqe,bhke->bhqk', q, k) * scale
            scores = torch.where(mask[None, None], scores, -1000000000.0)
            p = _softmax(scores, axis=-1)
            o = torch.einsum('bhqk,bhke->bhqe', p, v)
            o = torch.einsum('bhte,hed->btd', o, Wo)
            mem = o[:, :M, :].clone()
            outs.append(o[:, M:, :])
        return torch.concatenate(outs, axis=1)
    a = _A_ref(A_x, A_wqkv, A_wo)
    b = _B_ref(B_x, B_mem0, B_Wq, B_Wk, B_Wv, B_Wo, B_seg_len)
    return torch.concatenate([a, b], axis=-1)
```

```cpp
#include <hip/hip_runtime.h>
#include <hip/hip_bf16.h>
#include <stdint.h>

constexpr int S_ = 2048, D_ = 1024;
constexpr int HA = 32, DHA = 32, WIN = 256;
constexpr int HB = 32, DHB = 128, MEMR = 8, SEG = 32, NSEG = 64;
constexpr int OUTLD = 2048;

typedef unsigned short u16;
typedef __attribute__((ext_vector_type(8))) short bf16x8;
typedef __attribute__((ext_vector_type(4))) short s16x4;
typedef __attribute__((ext_vector_type(4))) float f32x4;
typedef __attribute__((ext_vector_type(4))) unsigned u32x4;
typedef __attribute__((ext_vector_type(2))) unsigned u32x2;
typedef __attribute__((address_space(3))) s16x4 lds_s16x4;
typedef __attribute__((address_space(1))) unsigned gu32;
typedef __attribute__((address_space(1))) unsigned long long gu64;
#define RLX_AGENT __ATOMIC_RELAXED, __HIP_MEMORY_SCOPE_AGENT

__device__ __forceinline__ float bf2f(u16 x) { return __uint_as_float(((unsigned)x) << 16); }
__device__ __forceinline__ u16 f2bf(float f) {
  unsigned u = __float_as_uint(f);
  u += 0x7fffu + ((u >> 16) & 1u);
  return (u16)(u >> 16);
}
__device__ __forceinline__ float rbf(float f) { return bf2f(f2bf(f)); }
__device__ __forceinline__ unsigned pack2(float a, float b) { return (unsigned)f2bf(a) | ((unsigned)f2bf(b) << 16); }

struct GemmArgs {
  const u16* A; const u16* B0; const u16* B1; const u16* B2; u16* C;
  int M, N, K, lda, ldb, ldc, npanel;
  int pad_;
};

__device__ __forceinline__ unsigned offB(unsigned row, unsigned ch) {
  return 256u * row + 16u * (ch ^ (((row & 3) << 2) | ((row >> 2) & 3)));
}

__device__ __forceinline__ void gemm_tile(const GemmArgs& g, int wg, char* smem) {
  const int nM = g.M / 128, nN = g.N / 128, nwg = nM * nN;
  { int q = nwg / 8, r = nwg % 8, x = wg % 8, o = wg / 8;
    wg = (x < r ? x * (q + 1) : r * (q + 1) + (x - r) * q) + o; }
  const int pm = wg % nM, pn = wg / nM;
  const int m0 = pm * 128, n0 = pn * 128;
  const u16* Bp = g.B0; int nb = n0;
  if (n0 >= 2 * g.npanel) { Bp = g.B2; nb = n0 - 2 * g.npanel; }
  else if (n0 >= g.npanel) { Bp = g.B1; nb = n0 - g.npanel; }
  const int tid = threadIdx.x, lane = tid & 63, wid = tid >> 6;
  const int wr = wid >> 1, wc = wid & 1, fr = lane & 15, fq = lane >> 4;
  f32x4 acc[4][4];
  #pragma unroll
  for (int i = 0; i < 4; ++i)
    #pragma unroll
    for (int j = 0; j < 4; ++j) acc[i][j] = (f32x4){0.f, 0.f, 0.f, 0.f};
  const int nt = g.K / 64;

  auto stage = [&](int kt, int buf) {
    const int k0 = kt * 64;
    #pragma unroll
    for (int i = 0; i < 4; ++i) {
      int b = (wid * 4 + i) * 1024 + lane * 16;
      int row = b >> 7, pc = (b >> 4) & 7, ch = pc ^ (row & 7);
      const u16* src = g.A + (size_t)(m0 + row) * g.lda + k0 + ch * 8;
      __builtin_amdgcn_global_load_lds((const unsigned*)src, (unsigned*)(smem + buf * 16384 + (wid * 4 + i) * 1024), 16, 0, 0);
    }
    #pragma unroll
    for (int i = 0; i < 4; ++i) {
      int b = (wid * 4 + i) * 1024 + lane * 16;
      int row = b >> 8, pc = (b >> 4) & 15, ch = pc ^ (((row & 3) << 2) | ((row >> 2) & 3));
      const u16* src = Bp + (size_t)(k0 + row) * g.ldb + nb + ch * 8;
      __builtin_amdgcn_global_load_lds((const unsigned*)src, (unsigned*)(smem + 32768 + buf * 16384 + (wid * 4 + i) * 1024), 16, 0, 0);
    }
  };
  stage(0, 0);
  for (int kt = 0; kt < nt; ++kt) {
    asm volatile("s_waitcnt vmcnt(0)" ::: "memory");
    __syncthreads();
    if (kt + 1 < nt) stage(kt + 1, (kt + 1) & 1);
    const char* sa = smem + (kt & 1) * 16384;
    const char* sb = smem + 32768 + (kt & 1) * 16384;
    #pragma unroll
    for (int kk = 0; kk < 2; ++kk) {
      bf16x8 a[4], b[4];
      #pragma unroll
      for (int m = 0; m < 4; ++m) {
        int row = wr * 64 + m * 16 + fr, ch = kk * 4 + fq;
        a[m] = *(const bf16x8*)(sa + row * 128 + ((ch ^ (row & 7)) << 4));
      }
      #pragma unroll
      for (int n = 0; n < 4; ++n) {
        const int c = wc * 4 + n, q = (lane & 15) >> 2, p = lane & 3;
        s16x4 lo = __builtin_amdgcn_ds_read_tr16_b64_v4i16((lds_s16x4*)(sb + offB(32 * kk + 8 * fq + q, 2 * c + (p >> 1)) + 8 * (p & 1)));
        s16x4 hi = __builtin_amdgcn_ds_read_tr16_b64_v4i16((lds_s16x4*)(sb + offB(32 * kk + 8 * fq + 4 + q, 2 * c + (p >> 1)) + 8 * (p & 1)));
        b[n] = (bf16x8){lo[0], lo[1], lo[2], lo[3], hi[0], hi[1], hi[2], hi[3]};
      }
      #pragma unroll
      for (int m = 0; m < 4; ++m)
        #pragma unroll
        for (int n = 0; n < 4; ++n)
          acc[m][n] = __builtin_amdgcn_mfma_f32_16x16x32_bf16(a[m], b[n], acc[m][n], 0, 0, 0);
    }
  }
  #pragma unroll
  for (int m = 0; m < 4; ++m)
    #pragma unroll
    for (int n = 0; n < 4; ++n)
      #pragma unroll
      for (int j = 0; j < 4; ++j)
        g.C[(size_t)(m0 + wr * 64 + m * 16 + fq * 4 + j) * g.ldc + n0 + wc * 64 + n * 16 + fr] = f2bf(acc[m][n][j]);
  __syncthreads();
}
__global__ __launch_bounds__(256, 2) void gemm_bf16(GemmArgs g) {
  extern __shared__ __attribute__((aligned(16))) char smem[];
  gemm_tile(g, blockIdx.x, smem);
}

__device__ __forceinline__ void attnA_block(const u16* qkv, u16* oA, int bid, char* smem) {
  u16* vs = (u16*)smem;
  const int h = bid & 31, qb = bid >> 5;
  const int tid = threadIdx.x, lane = tid & 63, wid = tid >> 6;
  const int q0 = qb * 64;
  const int kbase = q0 - 256;
  for (int i = tid; i < 320 * 4; i += 256) {
    int r = i >> 2, c = i & 3, key = kbase + r;
    u32x4 v = (u32x4){0, 0, 0, 0};
    if (key >= 0) v = *(const u32x4*)(qkv + (size_t)key * 3072 + 2048 + h * 32 + c * 8);
    *(u32x4*)(vs + r * 32 + c * 8) = v;
  }
  __syncthreads();
  const int fr = lane & 15, fq = lane >> 4;
  const int qw = q0 + wid * 16;
  const int myq = qw + fr;
  bf16x8 qf = *(const bf16x8*)(qkv + (size_t)myq * 3072 + h * 32 + fq * 8);
  const float scale = 0.17677669529663687f;
  float mrun = -3.0e38f, lrun = 0.f;
  f32x4 o[2] = {{0, 0, 0, 0}, {0, 0, 0, 0}};
  const int tlo = (wid * 16 + 1) >> 5;
  const int thi = (wid * 16 + 271) >> 5;
  for (int tp = tlo; tp <= thi; ++tp) {
    f32x4 s[2];
    #pragma unroll
    for (int u = 0; u < 2; ++u) {
      int key = kbase + tp * 32 + u * 16 + fr;
      int keyc = key < 0 ? 0 : key;
      bf16x8 kf = *(const bf16x8*)(qkv + (size_t)keyc * 3072 + 1024 + h * 32 + fq * 8);
      s[u] = __builtin_amdgcn_mfma_f32_16x16x32_bf16(kf, qf, (f32x4){0, 0, 0, 0}, 0, 0, 0);
    }
    float mx = -3.0e38f;
    #pragma unroll
    for (int u = 0; u < 2; ++u)
      #pragma unroll
      for (int r = 0; r < 4; ++r) {
        int key = kbase + tp * 32 + u * 16 + fq * 4 + r;
        float v = rbf(rbf(s[u][r]) * scale);
        bool ok = (key >= 0) && (myq - key >= 0) && (myq - key < WIN);
        v = ok ? v : -3.0e38f;
        s[u][r] = v;
        mx = fmaxf(mx, v);
      }
    mx = fmaxf(mx, __shfl_xor(mx, 16));
    mx = fmaxf(mx, __shfl_xor(mx, 32));
    float mnew = fmaxf(mrun, mx);
    float corr = __expf(mrun - mnew);
    float psum = 0.f;
    unsigned pk[4];
    #pragma unroll
    for (int u = 0; u < 2; ++u) {
      float p0 = __expf(s[u][0] - mnew), p1 = __expf(s[u][1] - mnew), p2 = __expf(s[u][2] - mnew), p3 = __expf(s[u][3] - mnew);
      p0 = rbf(p0); p1 = rbf(p1); p2 = rbf(p2); p3 = rbf(p3);
      psum += p0 + p1 + p2 + p3;
      pk[u * 2] = pack2(p0, p1); pk[u * 2 + 1] = pack2(p2, p3);
    }
    psum += __shfl_xor(psum, 16);
    psum += __shfl_xor(psum, 32);
    lrun = lrun * corr + psum;
    mrun = mnew;
    bf16x8 pf;
    { u32x4 t = (u32x4){pk[0], pk[1], pk[2], pk[3]}; pf = *(bf16x8*)&t; }
    #pragma unroll
    for (int dt = 0; dt < 2; ++dt) {
      const int q = (lane & 15) >> 2, p = lane & 3;
      const u16* b0 = vs + (tp * 32 + 4 * fq + q) * 32 + dt * 16 + 4 * p;
      s16x4 lo = __builtin_amdgcn_ds_read_tr16_b64_v4i16((lds_s16x4*)b0);
      s16x4 hi = __builtin_amdgcn_ds_read_tr16_b64_v4i16((lds_s16x4*)(b0 + 16 * 32));
      bf16x8 vf = (bf16x8){lo[0], lo[1], lo[2], lo[3], hi[0], hi[1], hi[2], hi[3]};
      #pragma unroll
      for (int r = 0; r < 4; ++r) o[dt][r] *= corr;
      o[dt] = __builtin_amdgcn_mfma_f32_16x16x32_bf16(vf, pf, o[dt], 0, 0, 0);
    }
  }
  float inv = 1.0f / lrun;
  #pragma unroll
  for (int dt = 0; dt < 2; ++dt) {
    u32x2 w = (u32x2){pack2(o[dt][0] * inv, o[dt][1] * inv), pack2(o[dt][2] * inv, o[dt][3] * inv)};
    *(u32x2*)(oA + (size_t)myq * 1024 + h * 32 + dt * 16 + fq * 4) = w;
  }
  __syncthreads();
}

__global__ __launch_bounds__(256) void attnB(const u16* __restrict__ qkvB, const u16* __restrict__ kmem,
                                             const u16* __restrict__ vmem, u16* __restrict__ oB) {
  __shared__ __attribute__((aligned(16))) u16 vs[4][48 * 136];
  const int tid = threadIdx.x, lane = tid & 63, wid = tid >> 6;
  const int task = blockIdx.x * 4 + wid;
  const int sgm = task >> 5, h = task & 31;
  const int fr = lane & 15, fq = lane >> 4;
  u16* v = vs[wid];
  for (int i = lane; i < 48 * 16; i += 64) {
    int r = i >> 4, c = i & 15;
    u32x4 val = (u32x4){0, 0, 0, 0};
    if (r < 8) val = *(const u32x4*)(vmem + ((size_t)sgm * 8 + r) * 4096 + h * 128 + c * 8);
    else if (r < 40) val = *(const u32x4*)(qkvB + (size_t)(sgm * 32 + r - 8) * 12288 + 8192 + h * 128 + c * 8);
    *(u32x4*)(v + r * 136 + c * 8) = val;
  }
  __builtin_amdgcn_wave_barrier();
  asm volatile("s_waitcnt lgkmcnt(0)" ::: "memory");
  const float scale = 0.08838834764831845f;
  #pragma unroll 1
  for (int qt = 0; qt < 2; ++qt) {
    const int myq = qt * 16 + fr;
    bf16x8 qf[4];
    #pragma unroll
    for (int ks = 0; ks < 4; ++ks)
      qf[ks] = *(const bf16x8*)(qkvB + (size_t)(sgm * 32 + myq) * 12288 + h * 128 + ks * 32 + fq * 8);
    f32x4 s[3];
    #pragma unroll
    for (int kt = 0; kt < 3; ++kt) {
      s[kt] = (f32x4){0, 0, 0, 0};
      int kr = kt * 16 + fr;
      const u16* kp;
      if (kr < 8) kp = kmem + ((size_t)sgm * 8 + kr) * 4096 + h * 128;
      else { int kx = kr < 40 ? kr - 8 : 0; kp = qkvB + (size_t)(sgm * 32 + kx) * 12288 + 4096 + h * 128; }
      #pragma unroll
      for (int ks = 0; ks < 4; ++ks) {
        bf16x8 kf = *(const bf16x8*)(kp + ks * 32 + fq * 8);
        s[kt] = __builtin_amdgcn_mfma_f32_16x16x32_bf16(kf, qf[ks], s[kt], 0, 0, 0);
      }
    }
    float mx = -3.0e38f;
    #pragma unroll
    for (int kt = 0; kt < 3; ++kt)
      #pragma unroll
      for (int r = 0; r < 4; ++r) {
        int key = kt * 16 + fq * 4 + r;
        float val = rbf(rbf(s[kt][r]) * scale);
        bool ok = (key < 8) || (key - 8 <= myq && key < 40);
        val = ok ? val : -3.0e38f;
        s[kt][r] = val;
        mx = fmaxf(mx, val);
      }
    mx = fmaxf(mx, __shfl_xor(mx, 16));
    mx = fmaxf(mx, __shfl_xor(mx, 32));
    float psum = 0.f;
    float p[3][4];
    #pragma unroll
    for (int kt = 0; kt < 3; ++kt)
      #pragma unroll
      for (int r = 0; r < 4; ++r) { p[kt][r] = __expf(s[kt][r] - mx); psum += p[kt][r]; }
    psum += __shfl_xor(psum, 16);
    psum += __shfl_xor(psum, 32);
    float inv = 1.0f / psum;
    u32x4 pa = (u32x4){pack2(p[0][0] * inv, p[0][1] * inv), pack2(p[0][2] * inv, p[0][3] * inv),
                       pack2(p[1][0] * inv, p[1][1] * inv), pack2(p[1][2] * inv, p[1][3] * inv)};
    u32x4 pb = (u32x4){pack2(p[2][0] * inv, p[2][1] * inv), pack2(p[2][2] * inv, p[2][3] * inv), 0u, 0u};
    bf16x8 pfa = *(bf16x8*)&pa, pfb = *(bf16x8*)&pb;
    #pragma unroll
    for (int dt = 0; dt < 8; ++dt) {
      const int q = (lane & 15) >> 2, pp = lane & 3;
      const u16* b0 = v + (4 * fq + q) * 136 + dt * 16 + 4 * pp;
      s16x4 l0 = __builtin_amdgcn_ds_read_tr16_b64_v4i16((lds_s16x4*)b0);
      s16x4 l1 = __builtin_amdgcn_ds_read_tr16_b64_v4i16((lds_s16x4*)(b0 + 16 * 136));
      s16x4 l2 = __builtin_amdgcn_ds_read_tr16_b64_v4i16((lds_s16x4*)(b0 + 32 * 136));
      bf16x8 vfa = (bf16x8){l0[0], l0[1], l0[2], l0[3], l1[0], l1[1], l1[2], l1[3]};
      bf16x8 vfb = (bf16x8){l2[0], l2[1], l2[2], l2[3], 0, 0, 0, 0};
      f32x4 o = __builtin_amdgcn_mfma_f32_16x16x32_bf16(vfa, pfa, (f32x4){0, 0, 0, 0}, 0, 0, 0);
      o = __builtin_amdgcn_mfma_f32_16x16x32_bf16(vfb, pfb, o, 0, 0, 0);
      u32x2 w = (u32x2){pack2(o[0], o[1]), pack2(o[2], o[3])};
      *(u32x2*)(oB + (size_t)(sgm * 32 + myq) * 4096 + h * 128 + dt * 16 + fq * 4) = w;
    }
  }
}

struct ChainWs {
  unsigned long long g1[2][32][8][192];
  unsigned long long gA[2][256][32][32];
  unsigned long long gB[2][4096];
  unsigned tmo[64];
  unsigned cnt[64];
};
constexpr unsigned kSpinCap = 4000000;
#define STAMP(i)

template <int N>
__device__ __forceinline__ void sweep(gu64* g, int stride, unsigned epoch, unsigned (&v)[N], unsigned* tmo) {
  unsigned spins = 0;
  while (true) {
    bool ok = true;
    #pragma unroll
    for (int k = 0; k < N; ++k) {
      unsigned long long x = __hip_atomic_load(g + k * stride, RLX_AGENT);
      v[k] = (unsigned)x; ok &= (unsigned)(x >> 32) == epoch;
    }
    if (__all(ok)) break;
    if (++spins > kSpinCap) { *tmo = 1; break; }
  }
}

struct WorkArgs { GemmArgs gq; GemmArgs ga; GemmArgs go; const u16* qkvA; u16* oA; };
__device__ __forceinline__ void wait_count(unsigned* c, unsigned target, unsigned* tmo) {
  if (threadIdx.x == 0) {
    unsigned spins = 0;
    while (__hip_atomic_load((gu32*)c, RLX_AGENT) < target) { __builtin_amdgcn_s_sleep(8); if (++spins > kSpinCap) { *tmo = 2; break; } }
    __builtin_amdgcn_fence(__ATOMIC_ACQUIRE, "agent");
    asm volatile("s_waitcnt vmcnt(0)" ::: "memory");
  }
  __syncthreads();
}
__device__ __forceinline__ void signal_count(unsigned* c) {
  asm volatile("s_waitcnt vmcnt(0)" ::: "memory");
  __syncthreads();
  if (threadIdx.x == 0) {
    __builtin_amdgcn_fence(__ATOMIC_RELEASE, "agent");
    asm volatile("s_waitcnt vmcnt(0)" ::: "memory");
    __hip_atomic_fetch_add((gu32*)c, 1u, RLX_AGENT);
  }
}
__global__ __launch_bounds__(256, 2) void chainB(const u16* __restrict__ mem0, const u16* __restrict__ Wq,
                                                 const u16* __restrict__ Wk, const u16* __restrict__ Wv,
                                                 const u16* __restrict__ Wo, const u16* __restrict__ qkvB,
                                                 u16* __restrict__ kmem, u16* __restrict__ vmem, ChainWs* w, WorkArgs wa) {
  extern __shared__ __attribute__((aligned(16))) char dsm[];
  if (blockIdx.x >= 256) {
    const int wk = blockIdx.x - 256;
    for (int t = wk; t < 384; t += 256) { gemm_tile(wa.ga, t, dsm); signal_count(&w->cnt[0]); }
    for (int t = wk; t < 512; t += 256) gemm_tile(wa.gq, t, dsm);
    wait_count(&w->cnt[0], 384, w->tmo);
    for (int t = wk; t < 1024; t += 256) { attnA_block(wa.qkvA, wa.oA, t, dsm); signal_count(&w->cnt[32]); }
    if (wk < 128) { wait_count(&w->cnt[32], 1024, w->tmo); gemm_tile(wa.go, wk, dsm); }
    return;
  }
  u16* smem_ = (u16*)dsm;
  float* red = (float*)(dsm + 16512);
  u16 (*qs)[8 * 136] = (u16 (*)[8 * 136])(dsm + 22656);
  u16 (*kx)[32 * 136] = (u16 (*)[32 * 136])(dsm + 29184);
  u16 (*vall)[48 * 136] = (u16 (*)[48 * 136])(dsm + 46592);
  float* rsum = (float*)(dsm + 72704);
  __builtin_amdgcn_s_setprio(3);
  const int b = blockIdx.x;
  const int h = (b % 8) * 4 + (b / 64);
  const int j = (b / 8) % 8;
  int tid = threadIdx.x, lane = tid & 63, wid = tid >> 6;
  int fr = lane & 15, fq = lane >> 4;

  bf16x8 w1[3][8];
  bf16x8 w2[8];
  {
    u16* L = (u16*)dsm + wid * 4480;
    #pragma unroll
    for (int t = 0; t < 3; ++t) {
      const u16* W = t == 0 ? Wq : (t == 1 ? Wk : Wv);
      #pragma unroll 4
      for (int i = lane; i < 4096; i += 64) {
        int k = i >> 4, c = i & 15;
        L[c * 264 + k] = W[(size_t)(256 * wid + k) * 4096 + h * 128 + j * 16 + c];
      }
      __builtin_amdgcn_wave_barrier();
      asm volatile("s_waitcnt lgkmcnt(0)" ::: "memory");
      #pragma unroll
      for (int ks = 0; ks < 8; ++ks) w1[t][ks] = *(const bf16x8*)(L + fr * 264 + 32 * ks + 8 * fq);
      asm volatile("s_waitcnt lgkmcnt(0)" ::: "memory");
      __builtin_amdgcn_wave_barrier();
    }
    #pragma unroll 4
    for (int i = lane; i < 4096; i += 64) {
      int e = i >> 5, c = i & 31, r = e & 31;
      int pos = (e & ~31) + 8 * ((r & 15) >> 2) + (r >> 4) * 4 + (r & 3);
      L[c * 136 + pos] = Wo[((size_t)h * 128 + e) * 1024 + j * 128 + 2 * wid * 16 + c];
    }
    __builtin_amdgcn_wave_barrier();
    asm volatile("s_waitcnt lgkmcnt(0)" ::: "memory");
    #pragma unroll
    for (int c2 = 0; c2 < 2; ++c2)
      #pragma unroll
      for (int ks = 0; ks < 4; ++ks) w2[c2 * 4 + ks] = *(const bf16x8*)(L + (c2 * 16 + fr) * 136 + 32 * ks + 8 * fq);
    asm volatile("s_waitcnt lgkmcnt(0)" ::: "memory");
  }
  __syncthreads();
  for (int i = tid; i < 2 * 8 * 136; i += 256) { int bb = i / (8 * 136), r = i % (8 * 136); vall[bb][40 * 136 + r] = 0; }
  for (int i = tid; i < 8 * 128; i += 256) {
    int r = i >> 7, c = i & 127;
    *(u32x4*)(smem_ + r * 1032 + c * 8) = *(const u32x4*)(mem0 + r * 1024 + c * 8);
  }
  auto load_kv = [&](int s, int buf) {
    for (int i = tid; i < 32 * 16; i += 256) {
      int r = i >> 4, c = i & 15;
      const u16* src = qkvB + (size_t)(s * 32 + r) * 12288 + h * 128 + c * 8;
      *(u32x4*)(kx[buf] + r * 136 + c * 8) = *(const u32x4*)(src + 4096);
      *(u32x4*)(vall[buf] + (8 + r) * 136 + c * 8) = *(const u32x4*)(src + 8192);
    }
  };
  load_kv(0, 0);
  __syncthreads();

  const float scale = 0.08838834764831845f;
  for (int s = 0; s < NSEG; ++s) {
    const unsigned epoch = s + 1;
    const int cb = s & 1;
    asm volatile("" : "+v"(tid));
    lane = tid & 63; wid = tid >> 6; fr = lane & 15; fq = lane >> 4;
    STAMP(0);
    u32x4 pfk[2], pfv[2];
    if (s + 1 < NSEG) {
      #pragma unroll
      for (int i2 = 0; i2 < 2; ++i2) {
        int i = tid + i2 * 256, r = i >> 4, c = i & 15;
        const u16* src = qkvB + (size_t)((s + 1) * 32 + r) * 12288 + h * 128 + c * 8;
        pfk[i2] = *(const u32x4*)(src + 4096);
        pfv[i2] = *(const u32x4*)(src + 8192);
      }
    }
    f32x4 a1[3] = {{0, 0, 0, 0}, {0, 0, 0, 0}, {0, 0, 0, 0}};
    #pragma unroll
    for (int ks = 0; ks < 8; ++ks) {
      bf16x8 mf = (bf16x8){0, 0, 0, 0, 0, 0, 0, 0};
      if (fr < 8) mf = *(const bf16x8*)(smem_ + fr * 1032 + 256 * wid + 32 * ks + 8 * fq);
      #pragma unroll
      for (int t = 0; t < 3; ++t) a1[t] = __builtin_amdgcn_mfma_f32_16x16x32_bf16(w1[t][ks], mf, a1[t], 0, 0, 0);
    }
    if (fr < 8) {
      #pragma unroll
      for (int t = 0; t < 3; ++t) *(f32x4*)(red + ((wid * 3 + t) * 8 + fr) * 16 + 4 * fq) = a1[t];
    }
    __syncthreads();
    if (tid < 192) {
      int t = tid / 64, row = (tid >> 3) & 7, ep = tid & 7;
      float x0 = 0.f, x1 = 0.f;
      #pragma unroll
      for (int ww = 0; ww < 4; ++ww) { x0 += red[((ww * 3 + t) * 8 + row) * 16 + ep * 2]; x1 += red[((ww * 3 + t) * 8 + row) * 16 + ep * 2 + 1]; }
      unsigned pk = pack2(x0, x1);
      __hip_atomic_store((gu64*)&w->g1[cb][h][j][tid], ((unsigned long long)epoch << 32) | pk, RLX_AGENT);
      if (t == 1) *(unsigned*)(kmem + ((size_t)s * 8 + row) * 4096 + h * 128 + j * 16 + ep * 2) = pk;
      if (t == 2) *(unsigned*)(vmem + ((size_t)s * 8 + row) * 4096 + h * 128 + j * 16 + ep * 2) = pk;
    }
    STAMP(1);
    {
      unsigned v[6];
      sweep<6>((gu64*)&w->g1[cb][h][0][0] + tid, 256, epoch, v, w->tmo);
      #pragma unroll
      for (int k = 0; k < 6; ++k) {
        int idx = tid + k * 256, jj = idx / 192, rem = idx % 192;
        int t = rem / 64, row = (rem >> 3) & 7, ep = rem & 7;
        if (t < 2) *(unsigned*)(qs[t] + row * 136 + jj * 16 + ep * 2) = v[k];
        else *(unsigned*)(vall[cb] + row * 136 + jj * 16 + ep * 2) = v[k];
      }
    }
    __syncthreads();
    STAMP(2);
    f32x4 sc[3];
    {
      bf16x8 qf[4];
      #pragma unroll
      for (int ks = 0; ks < 4; ++ks) {
        qf[ks] = (bf16x8){0, 0, 0, 0, 0, 0, 0, 0};
        if (fr < 8) qf[ks] = *(const bf16x8*)(qs[0] + fr * 136 + ks * 32 + fq * 8);
      }
      #pragma unroll
      for (int kt = 0; kt < 3; ++kt) {
        sc[kt] = (f32x4){0, 0, 0, 0};
        int kr = kt * 16 + fr;
        const u16* kp = kr < 8 ? qs[1] + kr * 136 : kx[cb] + (kr < 40 ? kr - 8 : 0) * 136;
        #pragma unroll
        for (int ks = 0; ks < 4; ++ks) {
          bf16x8 kf = *(const bf16x8*)(kp + ks * 32 + fq * 8);
          sc[kt] = __builtin_amdgcn_mfma_f32_16x16x32_bf16(kf, qf[ks], sc[kt], 0, 0, 0);
        }
      }
    }
    STAMP(6);
    float mx = -3.0e38f;
    #pragma unroll
    for (int kt = 0; kt < 3; ++kt)
      #pragma unroll
      for (int r = 0; r < 4; ++r) {
        int key = kt * 16 + fq * 4 + r;
        float val = rbf(rbf(sc[kt][r]) * scale);
        val = key < 40 ? val : -3.0e38f;
        sc[kt][r] = val; mx = fmaxf(mx, val);
      }
    mx = fmaxf(mx, __shfl_xor(mx, 16));
    mx = fmaxf(mx, __shfl_xor(mx, 32));
    float psum = 0.f;
    #pragma unroll
    for (int kt = 0; kt < 3; ++kt)
      #pragma unroll
      for (int r = 0; r < 4; ++r) { sc[kt][r] = __expf(sc[kt][r] - mx); psum += sc[kt][r]; }
    psum += __shfl_xor(psum, 16);
    psum += __shfl_xor(psum, 32);
    float inv = 1.0f / psum;
    u32x4 pa = (u32x4){pack2(sc[0][0] * inv, sc[0][1] * inv), pack2(sc[0][2] * inv, sc[0][3] * inv),
                       pack2(sc[1][0] * inv, sc[1][1] * inv), pack2(sc[1][2] * inv, sc[1][3] * inv)};
    u32x4 pb = (u32x4){pack2(sc[2][0] * inv, sc[2][1] * inv), pack2(sc[2][2] * inv, sc[2][3] * inv), 0u, 0u};
    bf16x8 pfa = *(bf16x8*)&pa, pfb = *(bf16x8*)&pb;
    STAMP(7);
    f32x4 d2[2] = {{0, 0, 0, 0}, {0, 0, 0, 0}};
    #pragma unroll
    for (int ks = 0; ks < 4; ++ks) {
      f32x4 ot[2];
      #pragma unroll
      for (int dt = 0; dt < 2; ++dt) {
        const int q = (lane & 15) >> 2, pp = lane & 3;
        const u16* b0 = vall[cb] + (4 * fq + q) * 136 + 32 * ks + dt * 16 + 4 * pp;
        s16x4 l0 = __builtin_amdgcn_ds_read_tr16_b64_v4i16((lds_s16x4*)b0);
        s16x4 l1 = __builtin_amdgcn_ds_read_tr16_b64_v4i16((lds_s16x4*)(b0 + 16 * 136));
        s16x4 l2 = __builtin_amdgcn_ds_read_tr16_b64_v4i16((lds_s16x4*)(b0 + 32 * 136));
        bf16x8 vfa = (bf16x8){l0[0], l0[1], l0[2], l0[3], l1[0], l1[1], l1[2], l1[3]};
        bf16x8 vfb = (bf16x8){l2[0], l2[1], l2[2], l2[3], 0, 0, 0, 0};
        ot[dt] = __builtin_amdgcn_mfma_f32_16x16x32_bf16(vfa, pfa, (f32x4){0, 0, 0, 0}, 0, 0, 0);
        ot[dt] = __builtin_amdgcn_mfma_f32_16x16x32_bf16(vfb, pfb, ot[dt], 0, 0, 0);
      }
      u32x4 ob = (u32x4){pack2(ot[0][0], ot[0][1]), pack2(ot[0][2], ot[0][3]), pack2(ot[1][0], ot[1][1]), pack2(ot[1][2], ot[1][3])};
      bf16x8 of = *(bf16x8*)&ob;
      if (fr >= 8) of = (bf16x8){0, 0, 0, 0, 0, 0, 0, 0};
      d2[0] = __builtin_amdgcn_mfma_f32_16x16x32_bf16(w2[ks], of, d2[0], 0, 0, 0);
      d2[1] = __builtin_amdgcn_mfma_f32_16x16x32_bf16(w2[4 + ks], of, d2[1], 0, 0, 0);
    }
    STAMP(8);
    STAMP(9);
    STAMP(3);
    if (fr < 8) {
      #pragma unroll
      for (int c2 = 0; c2 < 2; ++c2)
        #pragma unroll
        for (int r = 0; r < 4; ++r)
          __hip_atomic_store((gu64*)&w->gA[cb][j * 32 + (2 * wid + c2) * 4 + fq][h][r * 8 + fr],
                             ((unsigned long long)epoch << 32) | __float_as_uint(d2[c2][r]), RLX_AGENT);
    }
    if (s + 1 < NSEG) {
      #pragma unroll
      for (int i2 = 0; i2 < 2; ++i2) {
        int i = tid + i2 * 256, r = i >> 4, c = i & 15;
        *(u32x4*)(kx[cb ^ 1] + r * 136 + c * 8) = pfk[i2];
        *(u32x4*)(vall[cb ^ 1] + (8 + r) * 136 + c * 8) = pfv[i2];
      }
    }
    STAMP(4);
    {
      unsigned v[4];
      sweep<4>((gu64*)&w->gA[cb][b][0][0] + tid, 256, epoch, v, w->tmo);
      float x = __uint_as_float(v[0]) + __uint_as_float(v[1]) + __uint_as_float(v[2]) + __uint_as_float(v[3]);
      x += __shfl_xor(x, 32);
      if (lane < 32) rsum[wid * 32 + lane] = x;
    }
    __syncthreads();
    if (tid < 16) {
      int s0 = (tid & 1) * 16 + (tid >> 1);
      float x0 = rsum[s0] + rsum[32 + s0] + rsum[64 + s0] + rsum[96 + s0];
      float x1 = rsum[s0 + 8] + rsum[40 + s0] + rsum[72 + s0] + rsum[104 + s0];
      __hip_atomic_store((gu64*)&w->gB[cb][b * 16 + tid], ((unsigned long long)epoch << 32) | pack2(x0, x1), RLX_AGENT);
    }
    STAMP(5);
    if (s + 1 < NSEG) {
      #pragma unroll 1
      for (int half = 0; half < 2; ++half) {
        unsigned v[8];
        sweep<8>((gu64*)&w->gB[cb][half * 2048] + tid, 256, epoch, v, w->tmo);
        #pragma unroll
        for (int k = 0; k < 8; ++k) {
          int idx = half * 2048 + tid + k * 256, row = (idx >> 1) & 7, cp = (idx >> 4) * 2 + (idx & 1);
          *(unsigned*)(smem_ + row * 1032 + cp * 2) = v[k];
        }
      }
    }
    __syncthreads();
  }
}

extern "C" void kernel_launch(void* const* d_in, const int* in_sizes, int n_in,
                              void* d_out, int out_size, void* d_ws, size_t ws_size,
                              hipStream_t stream) {
  const u16* A_x = (const u16*)d_in[0];
  const u16* A_wqkv = (const u16*)d_in[1];
  const u16* A_wo = (const u16*)d_in[2];
  const u16* B_x = (const u16*)d_in[3];
  const u16* B_mem0 = (const u16*)d_in[4];
  const u16* B_Wq = (const u16*)d_in[5];
  const u16* B_Wk = (const u16*)d_in[6];
  const u16* B_Wv = (const u16*)d_in[7];
  const u16* B_Wo = (const u16*)d_in[8];
  u16* out = (u16*)d_out;
  char* ws = (char*)d_ws;
  size_t off = 0;
  ChainWs* cw = (ChainWs*)(ws + off); off += (sizeof(ChainWs) + 4095) / 4096 * 4096;
  u16* qkvA = (u16*)(ws + off); off += (size_t)S_ * 3072 * 2;
  u16* oA = (u16*)(ws + off); off += (size_t)S_ * 1024 * 2;
  u16* qkvB = (u16*)(ws + off); off += (size_t)S_ * 12288 * 2;
  u16* kmem = (u16*)(ws + off); off += (size_t)NSEG * 8 * 4096 * 2;
  u16* vmem = (u16*)(ws + off); off += (size_t)NSEG * 8 * 4096 * 2;
  u16* oB = (u16*)(ws + off); off += (size_t)S_ * 4096 * 2;

  hipMemsetAsync(cw, 0, sizeof(ChainWs), stream);
  hipFuncSetAttribute((const void*)gemm_bf16, hipFuncAttributeMaxDynamicSharedMemorySize, 65536);

  GemmArgs g;
  g.A = B_x; g.B0 = B_Wk; g.B1 = B_Wv; g.B2 = B_Wv; g.C = qkvB + 4096; g.M = S_; g.N = 8192; g.K = 1024;
  g.lda = 1024; g.ldb = 4096; g.ldc = 12288; g.npanel = 4096; g.pad_ = 0;
  gemm_bf16<<<(S_ / 128) * (8192 / 128), 256, 65536, stream>>>(g);
  WorkArgs wa;
  wa.gq = g; wa.gq.B0 = B_Wq; wa.gq.B1 = B_Wq; wa.gq.B2 = B_Wq; wa.gq.C = qkvB; wa.gq.N = 4096; wa.gq.npanel = 1 << 28;
  wa.ga = g; wa.ga.A = A_x; wa.ga.B0 = A_wqkv; wa.ga.B1 = A_wqkv; wa.ga.B2 = A_wqkv; wa.ga.C = qkvA; wa.ga.N = 3072;
  wa.ga.ldb = 3072; wa.ga.ldc = 3072; wa.ga.npanel = 1 << 28;
  wa.go = g; wa.go.A = oA; wa.go.B0 = A_wo; wa.go.B1 = A_wo; wa.go.B2 = A_wo; wa.go.C = out; wa.go.N = 1024;
  wa.go.ldb = 1024; wa.go.ldc = OUTLD; wa.go.npanel = 1 << 28;
  wa.qkvA = qkvA; wa.oA = oA;
  hipFuncSetAttribute((const void*)chainB, hipFuncAttributeMaxDynamicSharedMemorySize, 73728);
  chainB<<<512, 256, 73728, stream>>>(B_mem0, B_Wq, B_Wk, B_Wv, B_Wo, qkvB, kmem, vmem, cw, wa);
  attnB<<<2048 / 4, 256, 0, stream>>>(qkvB, kmem, vmem, oB);
  g.A = oB; g.B0 = B_Wo; g.B1 = B_Wo; g.B2 = B_Wo; g.C = out + 1024; g.M = S_; g.N = 1024; g.K = 4096;
  g.lda = 4096; g.ldb = 1024; g.ldc = OUTLD; g.npanel = 1 << 28;
  gemm_bf16<<<(S_ / 128) * (1024 / 128), 256, 65536, stream>>>(g);
}
```
